```python
import torch

def _softmax(x, axis=-1):
    return torch.softmax(x, dim=axis)
def _gelu(x):
    return torch.nn.functional.gelu(x, approximate="tanh")
def _silu(x):
    return torch.nn.functional.silu(x)
def _sigmoid(x):
    return torch.sigmoid(x)
def _layernorm(x, axis=-1, eps=1e-5):
    m = x.mean(dim=axis, keepdim=True)
    v = x.var(dim=axis, keepdim=True, unbiased=False)
    return (x - m) / torch.sqrt(v + eps)
def _scatter_add(tgt, idx, src, axis=0):
    return tgt.clone().index_add_(axis, idx, src)
def _segment_sum(src, seg, n):
    out = torch.zeros((n,) + src.shape[1:], dtype=src.dtype)
    return out.index_add_(0, seg, src)
def _pad(x, pw):
    flat = tuple(v for pair in reversed(pw) for v in pair)
    return torch.nn.functional.pad(x, flat)

kernel_name = 'concat_heads__gqa_block_cached__mha_block_cached__near_ridge'

def setup_inputs():

    _w = (lambda a: (lambda t: t.to(torch.bfloat16) if t.is_floating_point() else t)(torch.as_tensor(a)))
    def _A_setup():
        B, Sq, D = 16, 128, 640
        Hq, Hkv, Sk, Dh = 32, 32, 512, 192
        assert Hq % Hkv == 0, (Hq, Hkv)
        return {
            "x": _w((lambda *s: torch.randn(*s, dtype=torch.bfloat16) * 0.8)(B, Sq, D)),
            "k_cache": _w((lambda *s: torch.randn(*s, dtype=torch.bfloat16) * 0.8)(B, Hkv, Sk, Dh)),
            "v_cache": _w((lambda *s: torch.randn(*s, dtype=torch.bfloat16) * 0.05)(B, Hkv, Sk, Dh)),
            "wq": _w((lambda *s: torch.randn(*s, dtype=torch.bfloat16) * 0.8)(D, Hq * Dh) * (D ** -0.5)),
            "wo": _w((lambda *s: torch.randn(*s, dtype=torch.bfloat16) * 0.05)(Hq * Dh, D) * ((Hq * Dh) ** -0.5)),
        }
    _A_in = _A_setup()

    def _B_setup():
        B, Sq, D = 16, 128, 640
        H, Sk, Dh = 32, 512, 192
        return {
            "x": _w((lambda *s: torch.randn(*s, dtype=torch.bfloat16) * 0.8)(B, Sq, D)),
            "k_cache": _w((lambda *s: torch.randn(*s, dtype=torch.bfloat16) * 0.8)(B, H, Sk, Dh)),
            "v_cache": _w((lambda *s: torch.randn(*s, dtype=torch.bfloat16) * 0.05)(B, H, Sk, Dh)),
            "wq": _w((lambda *s: torch.randn(*s, dtype=torch.bfloat16) * 0.8)(D, H * Dh) * (D ** -0.5)),
            "wo": _w((lambda *s: torch.randn(*s, dtype=torch.bfloat16) * 0.05)(H * Dh, D) * ((H * Dh) ** -0.5)),
        }
    _B_in = _B_setup()

    return {
        "A_x": _A_in["x"],
        "A_k_cache": _A_in["k_cache"],
        "A_v_cache": _A_in["v_cache"],
        "A_wq": _A_in["wq"],
        "A_wo": _A_in["wo"],
        "B_x": _B_in["x"],
        "B_k_cache": _B_in["k_cache"],
        "B_v_cache": _B_in["v_cache"],
        "B_wq": _B_in["wq"],
        "B_wo": _B_in["wo"],
    }

def reference(A_x, A_k_cache, A_v_cache, A_wq, A_wo, B_x, B_k_cache, B_v_cache, B_wq, B_wo):
    def _A_ref(x, k_cache, v_cache, wq, wo):
        B, Sq, D = x.shape
        Hq, Dh = 32, 192
        Hkv = k_cache.shape[1]
        rep = Hq // Hkv
        q = (lambda x, *d: x.permute(*d))((x @ wq).reshape(B, Sq, Hq, Dh), 0, 2, 1, 3)
        k = (lambda x, n, axis: torch.repeat_interleave(x, n, dim=axis))(k_cache, rep, axis=1)
        v = (lambda x, n, axis: torch.repeat_interleave(x, n, dim=axis))(v_cache, rep, axis=1)
        scores = torch.einsum("bhqd,bhkd->bhqk", q, k) * (Dh ** -0.5)
        p = _softmax(scores, axis=-1)
        o = torch.einsum("bhqk,bhkd->bhqd", p, v)
        return (lambda x, *d: x.permute(*d))(o, 0, 2, 1, 3).reshape(B, Sq, Hq * Dh) @ wo

    def _B_ref(x, k_cache, v_cache, wq, wo):
        B, Sq, D = x.shape
        H, Dh = 32, 192
        q = (lambda x, *d: x.permute(*d))((x @ wq).reshape(B, Sq, H, Dh), 0, 2, 1, 3)
        scores = torch.einsum("bhqd,bhkd->bhqk", q, k_cache) * (Dh ** -0.5)
        p = _softmax(scores, axis=-1)
        o = torch.einsum("bhqk,bhkd->bhqd", p, v_cache)
        return (lambda x, *d: x.permute(*d))(o, 0, 2, 1, 3).reshape(B, Sq, H * Dh) @ wo

    a = _A_ref(A_x, A_k_cache, A_v_cache, A_wq, A_wo)
    b = _B_ref(B_x, B_k_cache, B_v_cache, B_wq, B_wo)
    return torch.concatenate([a, b], axis=-1)
```

```cpp
#include <hip/hip_runtime.h>
#include <stdint.h>
#include <utility>
#pragma clang diagnostic ignored "-Winline-asm"
#pragma clang diagnostic ignored "-Wunused-value"

#define DI __device__ __forceinline__
#define LDS3 __attribute__((address_space(3)))
typedef __bf16 bf16x8 __attribute__((ext_vector_type(8)));
typedef __bf16 bf16x2 __attribute__((ext_vector_type(2)));
typedef short s16x4 __attribute__((ext_vector_type(4)));
typedef float f32x2 __attribute__((ext_vector_type(2)));
typedef float f32x16 __attribute__((ext_vector_type(16)));
typedef unsigned u32x4 __attribute__((ext_vector_type(4)));
typedef unsigned u32x2 __attribute__((ext_vector_type(2)));
typedef __amdgpu_buffer_rsrc_t rsrc_t;

constexpr int NBATCH = 16, SQ = 128, DM = 640, NH = 32, SK = 512, DH = 192;
constexpr unsigned KVBYTES = SK * DH * 2;
constexpr unsigned WQROW = NH * DH * 2;
#define NG 2
constexpr unsigned SLOT = 12288, NSLOT = 6, RING = SLOT * NSLOT, K1_LDS = NG * (RING + 4 * 2048);
constexpr float QSCALE = 0.07216878364870322f * 1.4426950408889634f;
constexpr float THR = 8.0f;
#define LOOKA 9
#define W_AUX ""
#define X_AUX ""
#define KV_AUX " nt"

#define AB_W 1u
#define AB_X 1u
#define AB_WO 1u
#define AB_O 1u
#define AB_PFWO 1u
#define THROTTLE 2
#define AB_KV 1u
DI unsigned rfl(unsigned x) { return __builtin_amdgcn_readfirstlane(x); }
DI rsrc_t mk_rsrc(const void* p, unsigned bytes) { return __builtin_amdgcn_make_buffer_rsrc((void*)p, (short)0, (int)bytes, 0x00020000); }
DI bf16x8 lds_row(unsigned a) { return *(const LDS3 bf16x8*)a; }
DI bf16x8 lds_tr(unsigned a0, unsigned a1) {
  s16x4 lo = __builtin_amdgcn_ds_read_tr16_b64_v4i16((LDS3 s16x4*)a0);
  s16x4 hi = __builtin_amdgcn_ds_read_tr16_b64_v4i16((LDS3 s16x4*)a1);
  return __builtin_bit_cast(bf16x8, __builtin_shufflevector(lo, hi, 0, 1, 2, 3, 4, 5, 6, 7));
}
DI f32x16 mfma(bf16x8 a, bf16x8 b, f32x16 c) { return __builtin_amdgcn_mfma_f32_32x32x16_bf16(a, b, c, 0, 0, 0); }
DI unsigned cvtpk(float lo, float hi) { f32x2 v = {lo, hi}; return __builtin_bit_cast(unsigned, __builtin_convertvector(v, bf16x2)); }
DI bf16x8 pack8(const f32x16& x, int s, float c) {
  u32x4 w;
  w[0] = cvtpk(x[8 * s + 0] * c, x[8 * s + 1] * c); w[1] = cvtpk(x[8 * s + 2] * c, x[8 * s + 3] * c);
  w[2] = cvtpk(x[8 * s + 4] * c, x[8 * s + 5] * c); w[3] = cvtpk(x[8 * s + 6] * c, x[8 * s + 7] * c);
  return __builtin_bit_cast(bf16x8, w);
}
DI float half_swap_max(float v) {
  auto r = __builtin_amdgcn_permlane32_swap(__float_as_uint(v), __float_as_uint(v), false, false);
  return fmaxf(__uint_as_float(r[0]), __uint_as_float(r[1]));
}
DI float half_swap_sum(float v) {
  auto r = __builtin_amdgcn_permlane32_swap(__float_as_uint(v), __float_as_uint(v), false, false);
  return __uint_as_float(r[0]) + __uint_as_float(r[1]);
}
template <int OFF> DI void dma1(unsigned m0v, unsigned v, rsrc_t srd, unsigned soff) {
  asm volatile("s_mov_b32 m0, %0\n\ts_nop 0\n\tbuffer_load_dwordx4 %1, %2, %3 offen offset:%4" W_AUX " lds" :: "s"(m0v), "v"(v), "s"(srd), "s"(soff), "i"(OFF) : "m0");
}
template <int OFF> DI void dma1_nt(unsigned m0v, unsigned v, rsrc_t srd, unsigned soff) {
  asm volatile("s_mov_b32 m0, %0\n\ts_nop 0\n\tbuffer_load_dwordx4 %1, %2, %3 offen offset:%4" KV_AUX " lds" :: "s"(m0v), "v"(v), "s"(srd), "s"(soff), "i"(OFF) : "m0");
}
#define PF_AUX ""
DI void dma1_pf(unsigned m0v, unsigned v, rsrc_t srd, unsigned soff) {
  asm volatile("s_mov_b32 m0, %0\n\ts_nop 0\n\tbuffer_load_dwordx4 %1, %2, %3 offen" PF_AUX " lds" :: "s"(m0v), "v"(v), "s"(srd), "s"(soff) : "m0");
}
template <int OFF> DI void gload_sc1(u32x4& dst, unsigned voff, rsrc_t srd, unsigned soff) {
  asm volatile("buffer_load_dwordx4 %0, %1, %2, %3 offen offset:%4 sc1" : "=&v"(dst) : "v"(voff), "s"(srd), "s"(soff), "i"(OFF));
}
template <int OFF> DI void gload(u32x4& dst, unsigned voff, rsrc_t srd, unsigned soff) {
  asm volatile("buffer_load_dwordx4 %0, %1, %2, %3 offen offset:%4" X_AUX : "=&v"(dst) : "v"(voff), "s"(srd), "s"(soff), "i"(OFF));
}
#define SBARRIER "s_barrier"
template <int N> DI void bar() { asm volatile("s_waitcnt vmcnt(%0)\n\t" SBARRIER :: "i"(N) : "memory"); }
template <int N> DI void bar4(u32x4 (&x)[4]) {
  asm volatile("s_waitcnt vmcnt(%4)\n\t" SBARRIER : "+v"(x[0]), "+v"(x[1]), "+v"(x[2]), "+v"(x[3]) : "i"(N) : "memory");
}
DI void l2pf(unsigned& dummy, unsigned voff, rsrc_t srd, unsigned soff) {
  asm volatile("buffer_load_dword %0, %1, %2, %3 offen" : "+v"(dummy) : "v"(voff), "s"(srd), "s"(soff));
}
#define GAP() __builtin_amdgcn_sched_barrier(0)
DI unsigned pin(unsigned x) { asm("" : "+v"(x)); return x; }
DI unsigned opaque(unsigned x) { asm volatile("" : "+v"(x)); return x; }

#define STAMP_AT(slot)
constexpr int PD = 6;
struct K1 {
  unsigned rd, wr, m0w, gring;
  unsigned cv[3], vx;
  unsigned k0, v0, w0;
  unsigned ad[4];
  rsrc_t sK, sV, sW, sX;
  rsrc_t nA, nB;
  unsigned nv[3], nso[5];
  rsrc_t pfs; unsigned pfm0;
  unsigned rot;
  f32x16 acc[6];
  bf16x8 qf[12];
  u32x4 xr[4][4];
  bf16x8 pre[PD];
  float m, l;
};
DI unsigned adv(unsigned s) { s += SLOT; return s == RING ? 0u : s; }
enum { TW = 0, TK = 1, TV = 2, TN = 3, TW2 = 4, TX = 5 };
template <int KIND> DI void set_ad(K1& c) {
  if constexpr (KIND != TW2) c.rd = adv(c.rd);
  const unsigned b = (KIND == TK ? c.k0 : KIND == TV ? c.v0 : c.w0) + (c.rd + (KIND == TW2 ? RING : 0u));
  c.ad[0] = pin(b);
  c.ad[1] = pin(b ^ (KIND == TK ? 32u : 64u));
  c.ad[2] = pin(b ^ (KIND == TK ? 64u : KIND == TV ? 32u : 16u));
  c.ad[3] = pin(b ^ (KIND == TK ? 96u : KIND == TV ? 96u : 80u));
}
template <int KIND> DI bf16x8 rdA(const K1& c, int f) {
  if constexpr (KIND == TK) return lds_row(c.ad[f & 3] + 128 * (f >> 2));
  else {
    const int s = f / 6, t = f % 6; const unsigned o = (16 * s) * 384 + 128 * (t >> 1);
    return lds_tr(c.ad[t & 1] + o, c.ad[2 + (t & 1)] + o + (KIND == TV ? 8 : 4) * 384);
  }
}
template <int KIND, int P> DI void piece(K1& c, rsrc_t srd, int idx) {
  if constexpr (KIND == TW) dma1<1024 * P>(c.m0w + c.wr, c.cv[P], srd, (unsigned)idx * (32u * NG) * WQROW);
  else if constexpr (KIND == TX) dma1_nt<1024 * P>(c.m0w + c.wr, c.nv[P], srd, c.nso[idx]);
  else if constexpr (KIND != TN) dma1_nt<1024 * P>(c.m0w + c.wr, c.cv[P], srd, (((unsigned)idx + c.rot) & 15u) * SLOT);
}
template <int KIND> DI void tile3(K1& c, rsrc_t srd, int idx) { piece<KIND, 0>(c, srd, idx); piece<KIND, 1>(c, srd, idx); piece<KIND, 2>(c, srd, idx); c.wr = adv(c.wr); }
template <int J, int I> DI void issueX1(K1& c, rsrc_t srd) { gload<1024 * I>(c.xr[J % 4][I], c.vx, srd, 4096u * J); }
template <int J> DI void issueX(K1& c, rsrc_t srd) { issueX1<J, 0>(c, srd); issueX1<J, 1>(c, srd); issueX1<J, 2>(c, srd); issueX1<J, 3>(c, srd); }
template <int CUR, int NXT, int DK, bool RT = false, class BAR, class MM, class EX>
DI void tile12(K1& c, rsrc_t dsrd, int didx, BAR&& barf, MM&& mm, EX&& extra, bool live = true) {
  bf16x8 a[12];
#pragma unroll
  for (int i = 0; i < PD; ++i) a[i] = c.pre[i];
#pragma unroll
  for (int f = 0; f < 12; ++f) {
    if (f + PD == 12) {
      if constexpr (NXT != TN) { if (!RT || live) { barf(); set_ad<NXT>(c); } else c.rd = adv(c.rd); } else c.rd = adv(c.rd);
    }
    if (f + PD < 12) a[f + PD] = rdA<CUR>(c, f + PD);
    else if constexpr (NXT != TN) { if (!RT || live) c.pre[f + PD - 12] = rdA<NXT>(c, f + PD - 12); }
    if constexpr (NXT != TN && DK != TN) {
      if (!RT || live) {
        if (f == 12 - PD + 1 && (DK == TK || DK == TV)) asm volatile("s_waitcnt vmcnt(%0)" :: "i"(THROTTLE));
        if (f == 12 - PD + 1) piece<DK, 0>(c, dsrd, didx);
        if (f == 12 - PD + 3) piece<DK, 1>(c, dsrd, didx);
        if (f == 12 - PD + 5) { piece<DK, 2>(c, dsrd, didx); c.wr = adv(c.wr); }
      }
    }
    extra(f);
    mm(f, a[f]);
    GAP();
  }
}

constexpr int nwait(int j) { return j == 1 ? 19 : j == 2 ? 8 : j <= 8 ? 12 : j == 9 ? 8 : j == 10 ? 16 : j == 11 ? 11 : 9; }
DI rsrc_t nsrd(const K1& c, int t) { return (t == 2 || t == 4) ? c.nB : c.nA; }
template <int HS> DI void qstep(K1& c, bool doA, bool first) {
  constexpr int N = HS + 1;
  constexpr bool ODD = HS & 1;
  constexpr int J = HS >> 1, JN = N >> 1;
  constexpr int M = ODD ? JN : J;
  constexpr int PK = M + 4 <= 9 ? TW : TX, PI = M + 4 <= 9 ? M + 4 : M + 4 - 10;
  tile12<(ODD ? TW2 : TW), (!ODD ? TW2 : N <= 19 ? TW : TK), (HS == 19 ? TX : TN), HS == 19>(c, c.nB, 4,
      [&]() {
        if constexpr (HS == 1) {
          if (first) bar<13>(); else bar<19>();
          asm volatile("" : "+v"(c.xr[1][0]), "+v"(c.xr[1][1]), "+v"(c.xr[1][2]), "+v"(c.xr[1][3]));
        } else if constexpr (ODD && JN <= 9) bar4<nwait(JN)>(c.xr[JN % 4]);
        else if constexpr (ODD) bar<nwait(JN)>();
      },
      [&](int f, bf16x8 a) { c.acc[f % 6] = mfma(a, __builtin_bit_cast(bf16x8, c.xr[J % 4][2 * (HS & 1) + f / 6]), c.acc[f % 6]); },
      [&](int f) {
        if constexpr (HS == 0) {
          if (f == 1) issueX1<2, 0>(c, c.sX);
          if (f == 3) issueX1<2, 1>(c, c.sX);
          if (f == 5) issueX1<2, 2>(c, c.sX);
          if (f == 7) issueX1<2, 3>(c, c.sX);
        } else if constexpr (ODD) {
          if constexpr (M + 2 <= 9) {
            if (f == 7) issueX1<M + 2, 0>(c, c.sX);
            if (f == 9) issueX1<M + 2, 1>(c, c.sX);
            if (f == 11) issueX1<M + 2, 2>(c, c.sX);
          }
        } else {
          if constexpr (M + 2 <= 9) { if (f == 1) issueX1<M + 2, 3>(c, c.sX); }
          const rsrc_t ps = PK == TW ? c.sW : nsrd(c, PI);
          if (f == 3) piece<PK, 0>(c, ps, PI);
          if (f == 5) piece<PK, 1>(c, ps, PI);
          if (f == 7) { piece<PK, 2>(c, ps, PI); c.wr = adv(c.wr); }
          if constexpr (M <= 8) { if (f == 9) dma1_pf(c.pfm0, c.vx, c.pfs, (M - 1) * 1024u); }
        }
      }, doA);
}
template <int... Js> DI void qphase(K1& c, bool doA, bool first, std::integer_sequence<int, Js...>) { (qstep<Js>(c, doA, first), ...); }

DI rsrc_t tsrd(const K1& c, int kind, int idx) { return kind == TX ? nsrd(c, idx) : kind == TK ? c.sK : c.sV; }
template <bool SM, int NXT, int DK> DI void qk_step(K1& c, f32x16& s, f32x16& sp, bf16x8& p0, float& ps, int didx) {
  s = f32x16{};
  if constexpr (SM) ps = 0.f;
  tile12<TK, NXT, DK>(c, tsrd(c, DK, didx), didx, [&]() { bar<9>(); },
      [&](int f, bf16x8 a) { s = mfma(a, c.qf[f], s); },
      [&](int f) {
        if constexpr (SM) {
          sp[f] = __builtin_amdgcn_exp2f(sp[f] - c.m); ps += sp[f];
          if (f == 8) p0 = pack8(sp, 0, 1.0f);
        }
      });
}
template <bool FIN, bool MX, int NXT, int DK>
DI void pv_step(K1& c, const bf16x8& p0, bf16x8& p1, f32x16& sp, float& ps, const f32x16& sn, int didx) {
  float mx = 0.f;
  tile12<TV, NXT, DK>(c, tsrd(c, DK, didx), didx, [&]() { bar<9>(); },
      [&](int f, bf16x8 a) { c.acc[f % 6] = mfma(a, f < 6 ? p0 : p1, c.acc[f % 6]); },
      [&](int f) {
        if constexpr (FIN) {
          if (f < 4) { sp[12 + f] = __builtin_amdgcn_exp2f(sp[12 + f] - c.m); ps += sp[12 + f]; }
          if (f == 4) { p1 = pack8(sp, 1, 1.0f); c.l += ps; }
        }
        if constexpr (MX) {
          if (f == 5) mx = fmaxf(fmaxf(sn[0], sn[1]), sn[2]);
          if (f >= 6 && f < 11) mx = fmaxf(fmaxf(mx, sn[2 * f - 9]), sn[2 * f - 8]);
          if (f == 11) mx = fmaxf(fmaxf(fmaxf(mx, sn[13]), sn[14]), sn[15]);
        }
      });
  if constexpr (MX) {
    if (__builtin_expect(__any(mx - c.m > THR), 0)) {
      const float mn = fmaxf(c.m, half_swap_max(mx));
      const float al = __builtin_amdgcn_exp2f(c.m - mn);
      c.m = mn; c.l *= al;
#pragma unroll
      for (int t = 0; t < 6; ++t)
#pragma unroll
        for (int r = 0; r < 16; ++r) c.acc[t][r] *= al;
    }
    GAP();
  }
}
#define LAG 0
DI void aphase(K1& c, unsigned gi, unsigned long long* stp, int sbase) {
  f32x16 sA, sB; bf16x8 p0, p1; float ps;
  if (LAG && gi == 1) asm volatile("s_barrier" ::: "memory");
  qk_step<false, TK, TK>(c, sA, sB, p0, ps, 3);
  {
    float mx = sA[0];
#pragma unroll
    for (int r = 1; r < 16; ++r) mx = fmaxf(mx, sA[r]);
    c.m = half_swap_max(mx); c.l = 0.f;
  }
  GAP();
#pragma unroll 1
  for (int i = 0; i < 12; i += 2) {
    STAMP_AT(sbase + (i >> 1));
    qk_step<true, TV, TV>(c, sB, sA, p0, ps, i + 2);
    pv_step<true, true, TK, TK>(c, p0, p1, sA, ps, sB, i + 4);
    qk_step<true, TV, TV>(c, sA, sB, p0, ps, i + 3);
    pv_step<true, true, TK, TK>(c, p0, p1, sB, ps, sA, i + 5);
  }
  qk_step<true, TV, TV>(c, sB, sA, p0, ps, 14);
  pv_step<true, true, TK, TV>(c, p0, p1, sA, ps, sB, 15);
  qk_step<true, TV, TX>(c, sA, sB, p0, ps, 0);
  pv_step<true, true, TK, TX>(c, p0, p1, sB, ps, sA, 1);
  qk_step<true, TV, TX>(c, sB, sA, p0, ps, 2);
  pv_step<true, true, TV, TX>(c, p0, p1, sA, ps, sB, 3);
  {
    ps = 0.f;
#pragma unroll
    for (int r = 0; r < 12; ++r) { sB[r] = __builtin_amdgcn_exp2f(sB[r] - c.m); ps += sB[r]; }
    p0 = pack8(sB, 0, 1.0f);
  }
  GAP();
  pv_step<true, false, TN, TN>(c, p0, p1, sB, ps, sB, 0);
  if (LAG && gi == 0) asm volatile("s_barrier" ::: "memory");
}

constexpr size_t SYNC_OFF = (size_t)112 << 20, SYNC_BYTES = 256 * 128 + 64 * 128;
constexpr size_t XFRAG = (size_t)116 << 20;
struct Args { const void* x[2]; const void* k[2]; const void* v[2]; const void* wq[2]; const void* wo[2]; void* o; void* out; };

constexpr unsigned T2 = 64 * 320, NS2 = 4, K2_LDS = 2 * NS2 * T2;
constexpr int K2_STEPS = 24;
constexpr size_t XCHG = (size_t)96 << 20;
constexpr size_t XFLAG = SYNC_OFF;
constexpr size_t OCNT = SYNC_OFF + 256 * 128;
struct K2 {
  unsigned m0w, base0, base2, vb; unsigned vsrc[5]; rsrc_t sW, sO;
  f32x16 acc[5]; u32x4 br[4][4]; bf16x8 pre[PD];
};
template <int R> DI bf16x8 rd2(const K2& c, int f) {
  const int s = f / 5, t = f % 5; const unsigned o = (R >= 2 ? c.base2 : c.base0) + (R & 1) * T2 + (16 * s) * 320 + 64 * t;
  return lds_tr(o, o + 8 * 320);
}
template <int R, int P> DI void piece2(K2& c, int j) {
  if constexpr (P < 4) dma1<1024 * P>(c.m0w + R * T2, c.vsrc[P], c.sW, (unsigned)j * 64u * (DM * 2u));
  else dma1<0>(c.m0w + R * T2 + 4096u, c.vsrc[4], c.sW, (unsigned)j * 64u * (DM * 2u));
}
template <int R, int I> DI void bload2(K2& c, int j) { gload_sc1<1024 * I>(c.br[R][I], c.vb, c.sO, (unsigned)j * 4096u); }
template <int R> DI void issue2b(K2& c, int j) { bload2<R, 0>(c, j); bload2<R, 1>(c, j); bload2<R, 2>(c, j); bload2<R, 3>(c, j); }
template <int R> DI void issue2w(K2& c, int j) { piece2<R, 0>(c, j); piece2<R, 1>(c, j); piece2<R, 2>(c, j); piece2<R, 3>(c, j); piece2<R, 4>(c, j); }
template <int R> DI void k2step(K2& c, int j) {
  constexpr int RN = (R + 1) & 3, RW = (R + 3) & 3;
  bf16x8 a[20];
#pragma unroll
  for (int i = 0; i < PD; ++i) a[i] = c.pre[i];
  const int jn = j + 3 < K2_STEPS ? j + 3 : j;
#pragma unroll
  for (int f = 0; f < 20; ++f) {
    if (f + PD == 20) bar4<8>(c.br[RN]);
    if (f + PD < 20) a[f + PD] = rd2<R>(c, f + PD); else c.pre[f + PD - 20] = rd2<RN>(c, f + PD - 20);
    if (f == 1) bload2<RW, 0>(c, jn);
    if (f == 4) bload2<RW, 1>(c, jn);
    if (f == 7) bload2<RW, 2>(c, jn);
    if (f == 10) bload2<RW, 3>(c, jn);
    if (f == 15) piece2<RW, 0>(c, jn);
    if (f == 16) piece2<RW, 1>(c, jn);
    if (f == 17) piece2<RW, 2>(c, jn);
    if (f == 18) piece2<RW, 3>(c, jn);
    if (f == 19) piece2<RW, 4>(c, jn);
    c.acc[f % 5] = mfma(a[f], __builtin_bit_cast(bf16x8, c.br[R][f / 5]), c.acc[f % 5]);
    GAP();
  }
}
typedef __attribute__((address_space(1))) unsigned gu32;
DI void oproj(const Args& args, char* lds) {
  const unsigned tid = threadIdx.x, lane = opaque(tid & 63u), w = rfl(tid >> 6), wq = w & 3u, kq = w >> 2;
  const unsigned r32 = lane & 31u, h = lane >> 5, q4 = (lane & 15u) >> 2, p4 = lane & 3u, blk = (lane >> 4) & 1u;
  const unsigned bid = blockIdx.x, xcd = bid & 7u, idx = bid >> 3;
  const unsigned kh = xcd & 1u, g = 8u * (xcd >> 1) + (idx >> 2), nt = idx & 3u, br = g >> 4, b = g & 15u;
  STAMP_AT(0);
  K2 c;
  const unsigned ldsb = (unsigned)(size_t)(LDS3 char*)lds + kq * (NS2 * T2);
  c.m0w = ldsb + 5120u * wq;
#pragma unroll
  for (int i = 0; i < 5; ++i) {
    const unsigned ci = 320u * wq + 64u * i + lane, row = ci / 20u, ch = ci % 20u;
    c.vsrc[i] = row * (DM * 2u) + 16u * ch - (i < 4 ? 1024u * i : 0u);
  }
  c.base0 = pin(ldsb + 320u * (4u * h + q4) + 32u * blk + 8u * p4);
  c.base2 = pin(c.base0 + 2u * T2);
  c.vb = lane * 16u;
  const unsigned k0 = kh * 3072u + kq * 1536u;
  c.sW = mk_rsrc((const char*)args.wo[br] + (size_t)k0 * (DM * 2u) + nt * 320u, AB_WO * (1536u * DM * 2u - nt * 320u));
  c.sO = mk_rsrc((const char*)args.o + ((size_t)(g * 4u + wq) * 384u + (k0 >> 4)) * 1024u, AB_O * 96u * 1024u);
#pragma unroll
  for (int t = 0; t < 5; ++t) c.acc[t] = f32x16{};
  issue2w<0>(c, 0); issue2w<1>(c, 1); issue2w<2>(c, 2);
  if (w == 0) {
    gu32* cnt = (gu32*)((char*)args.o + OCNT) + (g * 2u + kh) * 32u;
    for (unsigned spins = 0; spins < (1u << 22); ++spins) {
      if (__builtin_amdgcn_readfirstlane(__hip_atomic_load(cnt, __ATOMIC_RELAXED, __HIP_MEMORY_SCOPE_AGENT)) >= 16u) break;
      __builtin_amdgcn_s_sleep(1);
    }
  }
  asm volatile("s_barrier" ::: "memory");
  issue2b<0>(c, 0); issue2b<1>(c, 1); issue2b<2>(c, 2);
  bar4<8>(c.br[0]);
  STAMP_AT(1);
#pragma unroll
  for (int i = 0; i < PD; ++i) c.pre[i] = rd2<0>(c, i);
#pragma unroll 1
  for (int j = 0; j < K2_STEPS; j += 4) { STAMP_AT(2 + (j >> 2)); k2step<0>(c, j); k2step<1>(c, j + 1); k2step<2>(c, j + 2); k2step<3>(c, j + 3); }
  asm volatile("s_waitcnt vmcnt(0) lgkmcnt(0)\n\ts_barrier"
               : "+v"(c.br[0][0]), "+v"(c.br[0][1]), "+v"(c.br[0][2]), "+v"(c.br[0][3]), "+v"(c.br[1][0]), "+v"(c.br[1][1]), "+v"(c.br[1][2]), "+v"(c.br[1][3]),
                 "+v"(c.br[2][0]), "+v"(c.br[2][1]), "+v"(c.br[2][2]), "+v"(c.br[2][3]),
                 "+v"(c.br[3][0]), "+v"(c.br[3][1]), "+v"(c.br[3][2]), "+v"(c.br[3][3]) :: "memory");
  STAMP_AT(8);
  float* red = (float*)lds + (wq * 80u) * 64u + lane;
  if (kq == 0) {
#pragma unroll
    for (int t = 3; t < 5; ++t)
#pragma unroll
      for (int r = 0; r < 16; ++r) red[(t * 16 + r) * 64] = c.acc[t][r];
  } else {
#pragma unroll
    for (int t = 0; t < 3; ++t)
#pragma unroll
      for (int r = 0; r < 16; ++r) red[(t * 16 + r) * 64] = c.acc[t][r];
  }
  __syncthreads();
  if (kq == 0) {
#pragma unroll
    for (int t = 0; t < 3; ++t)
#pragma unroll
      for (int r = 0; r < 16; ++r) c.acc[t][r] += red[(t * 16 + r) * 64];
  } else {
#pragma unroll
    for (int t = 3; t < 5; ++t)
#pragma unroll
      for (int r = 0; r < 16; ++r) c.acc[t][r] += red[(t * 16 + r) * 64];
  }
  STAMP_AT(9);
  typedef float f32x4 __attribute__((ext_vector_type(4)));
  const unsigned l16 = lane * 16u;
  if (kq != kh) {
    const rsrc_t sX = mk_rsrc((char*)args.o + XCHG + (size_t)(bid * 4u + wq) * 12288u, 12288u);
#pragma unroll
    for (int t = 0; t < 5; ++t) {
      if ((kq == 0) != (t < 3)) continue;
#pragma unroll
      for (int gq = 0; gq < 4; ++gq) {
        const f32x4 v = {c.acc[t][4 * gq], c.acc[t][4 * gq + 1], c.acc[t][4 * gq + 2], c.acc[t][4 * gq + 3]};
        __builtin_amdgcn_raw_buffer_store_b128(__builtin_bit_cast(u32x4, v), sX, l16 + ((t % 3) * 4 + gq) * 1024u, 0, 16);
      }
    }
  }
  asm volatile("s_waitcnt vmcnt(0)" ::: "memory");
  __syncthreads();
  if (w == 0) {
    if (lane == 0) __hip_atomic_store((gu32*)((char*)args.o + XFLAG) + bid * 32u, 1u, __ATOMIC_RELAXED, __HIP_MEMORY_SCOPE_AGENT);
    gu32* flag = (gu32*)((char*)args.o + XFLAG) + (bid ^ 1u) * 32u;
    for (unsigned spins = 0; spins < (1u << 22); ++spins) {
      if (__builtin_amdgcn_readfirstlane(__hip_atomic_load(flag, __ATOMIC_RELAXED, __HIP_MEMORY_SCOPE_AGENT)) != 0u) break;
      __builtin_amdgcn_s_sleep(1);
    }
  }
  asm volatile("" ::: "memory");
  __syncthreads();
  if (kq == kh) {
    const rsrc_t sX = mk_rsrc((char*)args.o + XCHG + (size_t)((bid ^ 1u) * 4u + wq) * 12288u, 12288u);
    unsigned short* orow = (unsigned short*)args.out + (size_t)(b * SQ + 32u * wq + r32) * 1280u + br * 640u + nt * 160u + 4u * h;
    u32x4 p[12];
#pragma unroll
    for (int i = 0; i < 12; ++i) if (kq == 0 || i < 8) p[i] = __builtin_amdgcn_raw_buffer_load_b128(sX, l16 + i * 1024u, 0, 16);
#pragma unroll
    for (int t = 0; t < 5; ++t) {
      if ((kq == 0) != (t < 3)) continue;
#pragma unroll
      for (int gq = 0; gq < 4; ++gq) {
        const f32x4 pv = __builtin_bit_cast(f32x4, p[(t % 3) * 4 + gq]);
        const u32x2 o = {cvtpk(c.acc[t][4 * gq] + pv[0], c.acc[t][4 * gq + 1] + pv[1]), cvtpk(c.acc[t][4 * gq + 2] + pv[2], c.acc[t][4 * gq + 3] + pv[3])};
        *(u32x2*)(orow + 32 * t + 8 * gq) = o;
      }
    }
  }
  STAMP_AT(10);
}

static_assert(NG == 2 && K1_LDS == K2_LDS, "the output projection phase needs 8 waves and the whole LDS");
constexpr int K1_GRID = 512 / NG;
constexpr size_t QSTASH = (size_t)64 << 20;

__global__ __launch_bounds__(256 * NG, 2) void k1_qattn(Args args) {
  extern __shared__ __attribute__((aligned(16))) char lds[];
  const unsigned tid = threadIdx.x, lane = tid & 63u, w = rfl(tid >> 6), gi = w >> 2, wq = w & 3u;
  const unsigned r32 = lane & 31u, h = lane >> 5, q4 = (lane & 15u) >> 2, p4 = lane & 3u, blk = (lane >> 4) & 1u, qb = (q4 >> 1) & 1u;
  K1 c;
  const unsigned ldsb = (unsigned)(size_t)(LDS3 char*)lds;
  c.gring = gi * RING;
  c.m0w = ldsb + c.gring + 3072u * wq;
  auto voffs = [&](bool proj, unsigned (&v)[3]) {
    const unsigned ln = opaque(lane);
#pragma unroll
    for (int i = 0; i < 3; ++i) {
      const unsigned ci = 64u * i + ln, rl = ci / 24u, cp = ci % 24u, row = 8u * wq + rl;
      const unsigned f = (((row >> 1) & 1u) << 2) | ((row >> 2) & 3u), ch = cp ^ f;
      v[i] = (proj ? (32u * gi + row) * WQROW : row * 384u) + 16u * ch - 1024u * i;
    }
  };
  c.vx = lane * 16u;
  {
    const unsigned f = (((r32 >> 1) & 1u) << 2) | ((r32 >> 2) & 3u);
    c.k0 = ldsb + c.gring + 384u * r32 + 16u * (h ^ f);
    c.v0 = ldsb + c.gring + 384u * (4u * h + q4) + 64u * qb + 32u * blk + 16u * ((p4 >> 1) ^ h) + 8u * (p4 & 1u);
    c.w0 = ldsb + 384u * (8u * h + q4) + 64u * qb + 32u * (blk ^ h) + 16u * (p4 & 1u) + 8u * (p4 >> 1);
  }
  const unsigned bid = blockIdx.x, xcd = bid & 7u, idx = bid >> 3;
  const unsigned head = 8u * (xcd & 3u) + (idx & 7u), batch = 8u * (xcd >> 2) + NG * (idx >> 3) + gi;
#define ROT ((bid * 5u + (bid >> 4) + gi * 8u) & 15u)
  c.rot = ROT;
  auto srdW = [&](int br, bool live) { return mk_rsrc((const char*)args.wq[br] + head * 384u, live ? AB_W * (DM * WQROW - head * 384u) : 0u); };
  auto srdX = [&](int br) { return mk_rsrc((const char*)args.o + XFRAG + (size_t)((br * NBATCH + batch) * 4u + wq) * 40960u, AB_X * 40960u); };
  const size_t kvoff = (size_t)(batch * NH + head) * KVBYTES;
  auto srdK = [&](int br) { return mk_rsrc((const char*)args.k[br] + kvoff, AB_KV * KVBYTES); };
  auto srdV = [&](int br) { return mk_rsrc((const char*)args.v[br] + kvoff, AB_KV * KVBYTES); };
  auto next_is_attn = [&](int br) {
    c.nA = srdK(br); c.nB = srdV(br);
    voffs(false, c.nv);
    c.nso[0] = c.rot * SLOT; c.nso[1] = ((c.rot + 1u) & 15u) * SLOT; c.nso[2] = c.nso[0]; c.nso[3] = ((c.rot + 2u) & 15u) * SLOT; c.nso[4] = c.nso[1];
  };
  auto next_is_proj = [&](int br, bool live) {
    c.nA = srdW(br, live); c.nB = c.nA;
    voffs(true, c.nv);
#pragma unroll
    for (int t = 0; t < 5; ++t) c.nso[t] = t * (32u * NG) * WQROW;
  };
  STAMP_AT(0);
  c.rd = 0; c.wr = 0;
  c.sW = srdW(0, true);
  voffs(true, c.cv);
  tile3<TW>(c, c.sW, 0);
#pragma unroll
  for (int t = 0; t < 6; ++t) c.acc[t] = f32x16{};
  c.l = 1.f;
  auto storeO = [&](int item) {
    const float inv = __builtin_amdgcn_rcpf(half_swap_sum(c.l));
    const rsrc_t sO = mk_rsrc((char*)args.o + ((size_t)(((item < 0 ? 0 : item) * NBATCH + batch) * 4u + wq) * 384u + head * 12u) * 1024u, item >= 0 ? 12u * 1024u : 0u);
    const unsigned l16 = opaque(lane) * 16u;
#pragma unroll
    for (int t = 0; t < 6; ++t)
#pragma unroll
      for (int s = 0; s < 2; ++s)
        __builtin_amdgcn_raw_buffer_store_b128(__builtin_bit_cast(u32x4, pack8(c.acc[t], s, inv)), sO, l16 + (2 * t + s) * 1024u, 0, 16);
  };
#define STAGGER 0
  const bool late = STAGGER && NG == 2 && ((idx >> 3) & 1u);
  const rsrc_t sQ = mk_rsrc((char*)args.o + QSTASH + (size_t)(bid * 8u + w) * 12288u, 12288u);
  const int nseg = late ? 3 : 2;
  auto publishO = [&](int item) {
    if (tid < 2u) __hip_atomic_fetch_add((gu32*)((char*)args.o + OCNT) + ((item * NBATCH + (batch - gi) + tid) * 2u + (head >> 4)) * 32u, 1u, __ATOMIC_RELAXED, __HIP_MEMORY_SCOPE_AGENT);
  };
  int pendO = -1;
  int prevA = -1;
  int prevAcount = 0; (void)prevAcount;
#pragma unroll 1
  for (int seg = 0; seg < nseg; ++seg) {
    const int qi = seg < 2 ? seg : -1, ai = late ? (seg == 0 ? -1 : seg == 1 ? 1 : 0) : seg;
    if (qi >= 0) {
      c.sX = srdX(qi);
      issueX<0>(c, c.sX); issueX<1>(c, c.sX);
      GAP();
      storeO(prevA); pendO = prevA; prevA = -1;
      GAP();
      c.sW = srdW(qi, true);
      c.pfs = mk_rsrc((const char*)args.wo[gi] + (size_t)(bid * 4u + wq) * 7680u, seg == 1 ? AB_PFWO * 7680u : 0u);
      c.pfm0 = ldsb + 2u * RING + 2048u * w;
      voffs(true, c.cv);
      if (ai >= 0) next_is_attn(ai); else next_is_proj(qi + 1, true);
#pragma unroll
      for (int t = 0; t < 6; ++t) c.acc[t] = f32x16{};
      bar4<16>(c.xr[0]);
      STAMP_AT(1 + 3 * seg);
      if (seg == 0) { tile3<TW>(c, c.sW, 1); tile3<TW>(c, c.sW, 2); tile3<TW>(c, c.sW, 3); }
      tile3<TW>(c, c.sW, 4);
      c.rd = c.rd == 0 ? RING - SLOT : c.rd - SLOT;
      set_ad<TW>(c);
#pragma unroll
      for (int i = 0; i < PD; ++i) c.pre[i] = rdA<TW>(c, i);
      qphase(c, ai >= 0, seg == 0, std::make_integer_sequence<int, 20>{});
#pragma unroll
      for (int t = 0; t < 6; ++t) { c.qf[2 * t] = pack8(c.acc[t], 0, QSCALE); c.qf[2 * t + 1] = pack8(c.acc[t], 1, QSCALE); }
      if (pendO >= 0) { publishO(pendO); pendO = -1; }
      if (ai < 0) {
        const unsigned l16 = opaque(lane) * 16u;
#pragma unroll
        for (int f = 0; f < 12; ++f) __builtin_amdgcn_raw_buffer_store_b128(__builtin_bit_cast(u32x4, c.qf[f]), sQ, l16 + f * 1024u, 0, 0);
      }
    } else {
      storeO(prevA); pendO = prevA; prevA = -1;
      const unsigned l16 = opaque(lane) * 16u;
#pragma unroll
      for (int f = 0; f < 12; ++f) c.qf[f] = __builtin_bit_cast(bf16x8, __builtin_amdgcn_raw_buffer_load_b128(sQ, l16 + f * 1024u, 0, 0));
      asm volatile("" :: "v"(c.qf[0]), "v"(c.qf[1]), "v"(c.qf[2]), "v"(c.qf[3]), "v"(c.qf[4]), "v"(c.qf[5]), "v"(c.qf[6]), "v"(c.qf[7]),
                   "v"(c.qf[8]), "v"(c.qf[9]), "v"(c.qf[10]), "v"(c.qf[11]));
      STAMP_AT(1 + 3 * seg);
      bar<0>();
      tile3<TX>(c, c.nB, 4);
      c.rd = c.rd == 0 ? RING - SLOT : c.rd - SLOT;
      set_ad<TK>(c);
#pragma unroll
      for (int i = 0; i < PD; ++i) c.pre[i] = rdA<TK>(c, i);
    }
    STAMP_AT(2 + 3 * seg);
    if (ai >= 0) {
#pragma unroll
      for (int t = 0; t < 6; ++t) c.acc[t] = f32x16{};
      c.sK = srdK(ai); c.sV = srdV(ai);
      voffs(false, c.cv);
      if (seg + 1 < nseg) { if (late) next_is_attn(0); else next_is_proj(1, true); } else next_is_proj(0, false);
      aphase(c, gi, nullptr, 0);
      if (pendO >= 0) { publishO(pendO); pendO = -1; }
      prevA = ai;
      __builtin_amdgcn_s_setprio(0);
    }
    STAMP_AT(3 + 3 * seg);
  }
  storeO(prevA);
  asm volatile("s_waitcnt vmcnt(0) lgkmcnt(0)\n\ts_barrier" ::: "memory");
  publishO(prevA);
  oproj(args, lds);
}

__global__ __launch_bounds__(256) void k0_prep(Args args) {
  const unsigned tid = threadIdx.x, bid = blockIdx.x, lane = tid & 63u, w = tid >> 6;
  if (bid < (unsigned)(SYNC_BYTES / 4096)) ((u32x4*)((char*)args.o + SYNC_OFF))[bid * 256u + tid] = u32x4{0u, 0u, 0u, 0u};
  u32x4 xv[5];
#pragma unroll
  for (int i = 0; i < 5; ++i) {
    const unsigned fid = (bid * 4u + w) * 5u + i, ks = fid % 40u, rowblk = fid / 40u;
    const char* src = (const char*)args.x[rowblk >> 6] + ((size_t)((rowblk & 63u) * 32u + (lane & 31u)) * DM + ks * 16u + (lane >> 5) * 8u) * 2u;
    xv[i] = *(const u32x4*)src;
  }
#pragma unroll
  for (int i = 0; i < 5; ++i) *(u32x4*)((char*)args.o + XFRAG + (size_t)((bid * 4u + w) * 5u + i) * 1024u + lane * 16u) = xv[i];
}

extern "C" void kernel_launch(void* const* d_in, const int* in_sizes, int n_in, void* d_out, int out_size, void* d_ws, size_t ws_size,
                              hipStream_t stream) {
  (void)in_sizes; (void)n_in; (void)out_size; (void)ws_size;
  Args a;
  for (int br = 0; br < 2; ++br) {
    a.x[br] = d_in[5 * br + 0]; a.k[br] = d_in[5 * br + 1]; a.v[br] = d_in[5 * br + 2]; a.wq[br] = d_in[5 * br + 3]; a.wo[br] = d_in[5 * br + 4];
  }
  a.o = d_ws; a.out = d_out;
  hipFuncSetAttribute((const void*)k1_qattn, hipFuncAttributeMaxDynamicSharedMemorySize, (int)K1_LDS);
  hipLaunchKernelGGL(k0_prep, dim3(256), dim3(256), 0, stream, a);
  hipLaunchKernelGGL(k1_qattn, dim3(K1_GRID), dim3(256 * NG), K1_LDS, stream, a);
}
```
